```python
import torch

def _softmax(x, axis=-1):
    return torch.softmax(x, dim=axis)
def _gelu(x):
    return torch.nn.functional.gelu(x, approximate="tanh")
def _silu(x):
    return torch.nn.functional.silu(x)
def _sigmoid(x):
    return torch.sigmoid(x)
def _layernorm(x, axis=-1, eps=1e-5):
    m = x.mean(dim=axis, keepdim=True)
    v = x.var(dim=axis, keepdim=True, unbiased=False)
    return (x - m) / torch.sqrt(v + eps)
def _scatter_add(tgt, idx, src, axis=0):
    return tgt.clone().index_add_(axis, idx, src)
def _segment_sum(src, seg, n):
    out = torch.zeros((n,) + src.shape[1:], dtype=src.dtype)
    return out.index_add_(0, seg, src)
def _pad(x, pw):
    flat = tuple(v for pair in reversed(pw) for v in pair)
    return torch.nn.functional.pad(x, flat)

kernel_name = 'standalone__gqa_block_cached__near_ridge'

def setup_inputs():

    _w = (lambda a: (lambda t: t.to(torch.bfloat16) if t.is_floating_point() else t)(torch.as_tensor(a)))
    B, Sq, D = 8, 48, 1024
    Hq, Hkv, Sk, Dh = 32, 4, 2048, 128
    assert Hq % Hkv == 0, (Hq, Hkv)
    return {
        "x": _w((lambda *s: torch.randn(*s, dtype=torch.bfloat16) * 0.8)(B, Sq, D)),
        "k_cache": _w((lambda *s: torch.randn(*s, dtype=torch.bfloat16) * 0.8)(B, Hkv, Sk, Dh)),
        "v_cache": _w((lambda *s: torch.randn(*s, dtype=torch.bfloat16) * 0.05)(B, Hkv, Sk, Dh)),
        "wq": _w((lambda *s: torch.randn(*s, dtype=torch.bfloat16) * 0.8)(D, Hq * Dh) * (D ** -0.5)),
        "wo": _w((lambda *s: torch.randn(*s, dtype=torch.bfloat16) * 0.05)(Hq * Dh, D) * ((Hq * Dh) ** -0.5)),
    }

def reference(x, k_cache, v_cache, wq, wo):
    B, Sq, D = x.shape
    Hq, Dh = 32, 128
    Hkv = k_cache.shape[1]
    rep = Hq // Hkv
    q = (lambda x, *d: x.permute(*d))((x @ wq).reshape(B, Sq, Hq, Dh), 0, 2, 1, 3)
    k = (lambda x, n, axis: torch.repeat_interleave(x, n, dim=axis))(k_cache, rep, axis=1)
    v = (lambda x, n, axis: torch.repeat_interleave(x, n, dim=axis))(v_cache, rep, axis=1)
    scores = torch.einsum("bhqd,bhkd->bhqk", q, k) * (Dh ** -0.5)
    p = _softmax(scores, axis=-1)
    o = torch.einsum("bhqk,bhkd->bhqd", p, v)
    return (lambda x, *d: x.permute(*d))(o, 0, 2, 1, 3).reshape(B, Sq, Hq * Dh) @ wo
```

```cpp
#include <hip/hip_runtime.h>
#include <hip/hip_bf16.h>
#include <stdint.h>

constexpr int B = 8, SQ = 48, DM = 1024, HQ = 32, HKV = 4, SK = 2048, DH = 128;

__device__ __forceinline__ float bf2f(unsigned short u) { return __uint_as_float(((unsigned)u) << 16); }
__device__ __forceinline__ unsigned short f2bf(float f) { __bf16 b = (__bf16)f; return __builtin_bit_cast(unsigned short, b); }

__global__ void gemm_naive(const unsigned short* A, const unsigned short* Bm, unsigned short* C, int M, int N, int K) {
  __shared__ float As[16][65];
  __shared__ float Bs[64][17];
  int tx = threadIdx.x & 15, ty = threadIdx.x >> 4;
  int row = blockIdx.y * 16 + ty, col = blockIdx.x * 16 + tx;
  float acc = 0.f;
  for (int k0 = 0; k0 < K; k0 += 64) {
    for (int i = threadIdx.x; i < 16 * 64; i += 256) { int r = i >> 6, c = i & 63; As[r][c] = bf2f(A[(size_t)(blockIdx.y * 16 + r) * K + k0 + c]); }
    for (int i = threadIdx.x; i < 64 * 16; i += 256) { int r = i >> 4, c = i & 15; Bs[r][c] = bf2f(Bm[(size_t)(k0 + r) * N + blockIdx.x * 16 + c]); }
    __syncthreads();
    for (int k = 0; k < 64; ++k) acc += As[ty][k] * Bs[k][tx];
    __syncthreads();
  }
  C[(size_t)row * N + col] = f2bf(acc);
}

__global__ void attn_naive(const unsigned short* Q, const unsigned short* Kc, const unsigned short* Vc, unsigned short* O) {
  int row = blockIdx.x % SQ, h = (blockIdx.x / SQ) % HQ, b = blockIdx.x / (SQ * HQ);
  int hkv = h / (HQ / HKV);
  __shared__ float q[DH];
  __shared__ float s[SK];
  __shared__ float red[256];
  int t = threadIdx.x;
  if (t < DH) q[t] = bf2f(Q[((size_t)(b * SQ + row)) * (HQ * DH) + h * DH + t]);
  __syncthreads();
  const unsigned short* Kb = Kc + (size_t)(b * HKV + hkv) * SK * DH;
  const unsigned short* Vb = Vc + (size_t)(b * HKV + hkv) * SK * DH;
  float mx = -INFINITY;
  for (int k = t; k < SK; k += 256) {
    float a = 0.f;
    for (int d = 0; d < DH; ++d) a += q[d] * bf2f(Kb[(size_t)k * DH + d]);
    a *= 0.08838834764831845f;
    s[k] = a; mx = fmaxf(mx, a);
  }
  red[t] = mx; __syncthreads();
  for (int o = 128; o > 0; o >>= 1) { if (t < o) red[t] = fmaxf(red[t], red[t + o]); __syncthreads(); }
  mx = red[0]; __syncthreads();
  float sum = 0.f;
  for (int k = t; k < SK; k += 256) { float p = __expf(s[k] - mx); s[k] = p; sum += p; }
  red[t] = sum; __syncthreads();
  for (int o = 128; o > 0; o >>= 1) { if (t < o) red[t] += red[t + o]; __syncthreads(); }
  sum = red[0]; __syncthreads();
  int d = t & 127, half = t >> 7;
  float a = 0.f;
  for (int k = half; k < SK; k += 2) a += s[k] * bf2f(Vb[(size_t)k * DH + d]);
  red[t] = a; __syncthreads();
  if (t < 128) O[((size_t)(b * SQ + row)) * (HQ * DH) + h * DH + d] = f2bf((red[t] + red[t + 128]) / sum);
}

extern "C" void kernel_launch(void* const* d_in, const int* in_sizes, int n_in,
                              void* d_out, int out_size, void* d_ws, size_t ws_size,
                              hipStream_t stream) {
  const unsigned short* x = (const unsigned short*)d_in[0];
  const unsigned short* kc = (const unsigned short*)d_in[1];
  const unsigned short* vc = (const unsigned short*)d_in[2];
  const unsigned short* wq = (const unsigned short*)d_in[3];
  const unsigned short* wo = (const unsigned short*)d_in[4];
  unsigned short* q = (unsigned short*)d_ws;
  unsigned short* o = q + (size_t)B * SQ * HQ * DH;
  gemm_naive<<<dim3(HQ * DH / 16, B * SQ / 16), 256, 0, stream>>>(x, wq, q, B * SQ, HQ * DH, DM);
  attn_naive<<<B * HQ * SQ, 256, 0, stream>>>(q, kc, vc, o);
  gemm_naive<<<dim3(DM / 16, B * SQ / 16), 256, 0, stream>>>(o, wo, (unsigned short*)d_out, B * SQ, DM, HQ * DH);
}
```

```cpp
#include <hip/hip_runtime.h>
#include <hip/hip_bf16.h>
#include <stdint.h>

typedef __bf16 bf16x8 __attribute__((ext_vector_type(8)));
typedef __bf16 bf16x4 __attribute__((ext_vector_type(4)));
typedef __bf16 bf16x2 __attribute__((ext_vector_type(2)));
typedef float f32x4 __attribute__((ext_vector_type(4)));
typedef float f32x2 __attribute__((ext_vector_type(2)));
typedef unsigned u32x4 __attribute__((ext_vector_type(4)));
typedef unsigned u32x2 __attribute__((ext_vector_type(2)));
typedef unsigned long long u64;
#define DI __device__ __forceinline__
#define LAS __attribute__((address_space(3)))

constexpr int CL0 = 10, SG0 = 5, CL1 = 15, SG1 = 2, CL2 = 19, SG2 = 3, CL3 = 23, SG3 = 2;
constexpr int PDELAY = 3, PDELAY2 = 2;

constexpr int NB = 8, SQ = 48, DM = 1024, HQ = 32, HKV = 4, SK = 2048, DH = 128;
constexpr unsigned SLOT = 32768u, SCR = 131072u, LDS_BYTES = 163840u;
constexpr unsigned SCR_Q = SCR, SCR_ML = SCR + 12288u, SCR_DUMMY = SCR + 13824u;
constexpr unsigned SY_TAG = SCR + 14336u;
constexpr unsigned XOFF = 16384u;
constexpr size_t WS_E = 0, WS_C = 4096, WS_TMO = 20480;
constexpr size_t WS_O = 65536, WS_P = WS_O + (size_t)256 * 12288;
constexpr float QSCALE = 0.08838834764831845f * 1.4426950408889634f;
constexpr float L_LIMIT = 16777216.f;
constexpr float L_FLOOR = 5.9604645e-8f;
constexpr unsigned POISON = 0xffffffffu;
constexpr int SPIN_CAP = 40000;

DI unsigned rfl(unsigned x) { return __builtin_amdgcn_readfirstlane(x); }
DI unsigned cvtpk(float lo, float hi) { f32x2 v = {lo, hi}; bf16x2 b = __builtin_convertvector(v, bf16x2); return __builtin_bit_cast(unsigned, b); }
DI bf16x8 pack8(f32x4 a, f32x4 b) { u32x4 w = {cvtpk(a[0], a[1]), cvtpk(a[2], a[3]), cvtpk(b[0], b[1]), cvtpk(b[2], b[3])}; return __builtin_bit_cast(bf16x8, w); }
DI f32x4 mfma(bf16x8 a, bf16x8 b, f32x4 c) { return __builtin_amdgcn_mfma_f32_16x16x32_bf16(a, b, c, 0, 0, 0); }
DI bf16x8 lds128(LAS char* p) { return *(LAS bf16x8*)p; }
DI bf16x8 ldstr2(LAS char* p0, LAS char* p1) {
  bf16x4 a = __builtin_amdgcn_ds_read_tr16_b64_v4bf16((LAS bf16x4*)p0), b = __builtin_amdgcn_ds_read_tr16_b64_v4bf16((LAS bf16x4*)p1);
  return __builtin_shufflevector(a, b, 0, 1, 2, 3, 4, 5, 6, 7);
}
template <bool NT> DI void dma16(unsigned lds_addr, unsigned voff, __amdgpu_buffer_rsrc_t srd, unsigned soff) {
  if constexpr (NT) asm volatile("s_mov_b32 m0, %0\n\ts_nop 0\n\tbuffer_load_dwordx4 %1, %2, %3 offen nt lds" :: "s"(lds_addr), "v"(voff), "s"(srd), "s"(soff) : "memory");
  else              asm volatile("s_mov_b32 m0, %0\n\ts_nop 0\n\tbuffer_load_dwordx4 %1, %2, %3 offen lds" :: "s"(lds_addr), "v"(voff), "s"(srd), "s"(soff) : "memory");
}
DI void touch(unsigned lds_addr, unsigned voff, __amdgpu_buffer_rsrc_t srd, unsigned soff) {
  asm volatile("s_mov_b32 m0, %0\n\ts_nop 0\n\tbuffer_load_dword %1, %2, %3 offen lds" :: "s"(lds_addr), "v"(voff), "s"(srd), "s"(soff) : "memory");
}
template <int N> DI void wait_vm() { asm volatile("s_waitcnt vmcnt(%0)" :: "i"(N) : "memory"); }
DI unsigned sigma(unsigned u) { return (4u - u) & 3u; }
DI __amdgpu_buffer_rsrc_t mk_srd(const void* p, unsigned bytes) { return __builtin_amdgcn_make_buffer_rsrc((void*)p, (short)0, (int)bytes, 0x00020000); }
DI u64 ld_flag(const u64* p) { return __hip_atomic_load(p, __ATOMIC_RELAXED, __HIP_MEMORY_SCOPE_AGENT); }
DI void st_flag(u64* p, u64 v) { __hip_atomic_store(p, v, __ATOMIC_RELAXED, __HIP_MEMORY_SCOPE_AGENT); }
#define SB() __builtin_amdgcn_sched_barrier(0)
DI int fresh_lane() { int l; asm volatile("v_mbcnt_lo_u32_b32 %0, -1, 0\n\tv_mbcnt_hi_u32_b32 %0, -1, %0" : "=v"(l)); return l; }
DI float xmax(float t) { t = fmaxf(t, __shfl_xor(t, 16)); return fmaxf(t, __shfl_xor(t, 32)); }
DI float ex2(float x) { return __builtin_amdgcn_exp2f(x); }
DI bf16x8 asbf(u32x4 v) { return __builtin_bit_cast(bf16x8, v); }

DI u32x4 sanitize(u32x4 v) { return u32x4{min(v[0], POISON - 1u), min(v[1], POISON - 1u), min(v[2], POISON - 1u), min(v[3], POISON - 1u)}; }
DI unsigned max4(unsigned m, u32x4 v) { return max(max(m, max(v[0], v[1])), max(v[2], v[3])); }
DI void wait_words(u64 v0, const u64* p, bool mine, u64 tag, int lane, char* ws) {
  u64 v = v0;
  for (int spins = 0;; ++spins) {
    if (__all(!mine || v == tag)) break;
    if (spins > SPIN_CAP) { if (lane == 0) st_flag((u64*)(ws + WS_TMO), 1ull); break; }
    __builtin_amdgcn_s_sleep(1);
    v = mine ? ld_flag(p) : tag;
  }
}
DI void bar() { asm volatile("s_barrier" ::: "memory"); }
DI void bar_lgkm() { asm volatile("s_waitcnt lgkmcnt(0)\n\ts_barrier" ::: "memory"); }

template <int MODE>
DI void p2_step_plain(LAS char* kb_next, LAS char* vb0, LAS char* vb1, const bf16x8 (&Qf)[4][3], bf16x8 (&Kf)[2][4], f32x4 (&O)[8][3], f32x4 (&Ls)[3], f32x4& negm, float (&m)[3], bf16x8 ones) {
  f32x4 S[2][3];
  bf16x8 Vf[8];
  const f32x4 z = {0.f, 0.f, 0.f, 0.f};
#pragma unroll
  for (int q = 0; q < 3; ++q)
#pragma unroll
    for (int X = 0; X < 2; ++X) {
      S[X][q] = mfma(Kf[X][0], Qf[0][q], z);
#pragma unroll
      for (int s = 1; s < 4; ++s) S[X][q] = mfma(Kf[X][s], Qf[s][q], S[X][q]);
    }
  SB();
  bar_lgkm();
#pragma unroll
  for (int d = 0; d < 8; ++d) { LAS char* a = ((d & 1) ? vb1 : vb0) + 512 * (d >> 1); Vf[d] = ldstr2(a, a + 4096); }
  float t[3];
#pragma unroll
  for (int q = 0; q < 3; ++q)
    t[q] = xmax(fmaxf(fmaxf(fmaxf(S[0][q][0], S[0][q][1]), fmaxf(S[0][q][2], S[0][q][3])), fmaxf(fmaxf(S[1][q][0], S[1][q][1]), fmaxf(S[1][q][2], S[1][q][3]))));
  if constexpr (MODE == 0) {
    const float r = fmaxf(fmaxf(t[0], t[1]), t[2]);
    negm = f32x4{-r, -r, -r, -r};
#pragma unroll
    for (int q = 0; q < 3; ++q) { m[q] = r; S[0][q] -= r; S[1][q] -= r; }
  } else {
#pragma unroll
    for (int q = 0; q < 3; ++q) {
      const float mn = fmaxf(m[q], t[q]), al = ex2(m[q] - mn);
      m[q] = mn; Ls[q] *= al;
#pragma unroll
      for (int d = 0; d < 8; ++d) O[d][q] *= al;
      S[0][q] -= mn; S[1][q] -= mn;
    }
  }
  u32x4 Pw[3];
#pragma unroll
  for (int q = 0; q < 3; ++q)
    Pw[q] = u32x4{cvtpk(ex2(S[0][q][0]), ex2(S[0][q][1])), cvtpk(ex2(S[0][q][2]), ex2(S[0][q][3])), cvtpk(ex2(S[1][q][0]), ex2(S[1][q][1])), cvtpk(ex2(S[1][q][2]), ex2(S[1][q][3]))};
  SB();
#pragma unroll
  for (int q = 0; q < 3; ++q) {
#pragma unroll
    for (int d = 0; d < 8; ++d) O[d][q] = mfma(Vf[d], asbf(Pw[q]), O[d][q]);
    Ls[q] = mfma(ones, asbf(Pw[q]), Ls[q]);
  }
  SB();
  bar_lgkm();
#pragma unroll
  for (int X = 0; X < 2; ++X)
#pragma unroll
    for (int s = 0; s < 4; ++s) Kf[X][s] = lds128(kb_next + 4096 * X + 512 * s);
  SB();
}

DI void mfma_acc(f32x4& d, const bf16x8& a, const bf16x8& b) { asm volatile("v_mfma_f32_16x16x32_bf16 %0, %1, %2, %0" : "+v"(d) : "v"(a), "v"(b)); }
DI void mfma_init(f32x4& d, const bf16x8& a, const bf16x8& b, const f32x4& c) { asm volatile("v_mfma_f32_16x16x32_bf16 %0, %1, %2, %3" : "=&v"(d) : "v"(a), "v"(b), "v"(c)); }
DI void p2_step_fast(LAS char* kb_next, LAS char* vb0, LAS char* vb1, const bf16x8 (&Qf)[4][3], bf16x8 (&Kf)[2][4], f32x4 (&O)[8][3], f32x4 (&Ls)[3], const f32x4& negm, const bf16x8& ones) {
  f32x4 S[3][2];
  u32x4 Pw[3];
  bf16x4 Va[8], Vb[8];
#define QK(q, X, s) do { if ((s) == 0) mfma_init(S[q][X], Kf[X][0], Qf[0][q], negm); else mfma_acc(S[q][X], Kf[X][s], Qf[s][q]); } while (0)
#define EX(q, X, r) do { float e_ = ex2(S[q][X][r]); asm volatile("" : "+v"(e_)); S[q][X][r] = e_; } while (0)
#define CV(q, j) do { unsigned c_ = cvtpk(S[q][(j) >> 1][2 * ((j) & 1)], S[q][(j) >> 1][2 * ((j) & 1) + 1]); asm volatile("" : "+v"(c_)); Pw[q][j] = c_; } while (0)
#define VA(d) Va[d] = __builtin_amdgcn_ds_read_tr16_b64_v4bf16((LAS bf16x4*)((((d) & 1) ? vb1 : vb0) + 512 * ((d) >> 1)))
#define VB(d) Vb[d] = __builtin_amdgcn_ds_read_tr16_b64_v4bf16((LAS bf16x4*)((((d) & 1) ? vb1 : vb0) + 512 * ((d) >> 1) + 4096))
#define KR(X, s) Kf[X][s] = lds128(kb_next + 4096 * (X) + 512 * (s))
#define PV(q, d) mfma_acc(O[d][q], __builtin_shufflevector(Va[d], Vb[d], 0, 1, 2, 3, 4, 5, 6, 7), asbf(Pw[q]))
#define LS(q) mfma_acc(Ls[q], ones, asbf(Pw[q]))
  QK(0, 0, 0); VA(0); SB(); QK(0, 1, 0); VB(0); SB(); QK(0, 0, 1); VA(1); SB(); QK(0, 1, 1); VB(1); SB();
  QK(0, 0, 2); VA(2); SB(); QK(0, 1, 2); VB(2); SB(); QK(0, 0, 3); VA(3); SB(); QK(0, 1, 3); VB(3); SB();
  QK(1, 0, 0); SB(); QK(1, 1, 0); SB(); QK(1, 0, 1); EX(0, 0, 0); SB(); QK(1, 1, 1); EX(0, 0, 1); SB();
  QK(1, 0, 2); EX(0, 0, 2); SB(); QK(1, 1, 2); EX(0, 0, 3); SB(); QK(1, 0, 3); EX(0, 1, 0); SB(); QK(1, 1, 3); EX(0, 1, 1); SB();
  bar(); SB();
  QK(2, 0, 0); EX(0, 1, 2); SB(); QK(2, 1, 0); EX(0, 1, 3); SB(); QK(2, 0, 1); CV(0, 0); VA(4); SB(); QK(2, 1, 1); CV(0, 1); VB(4); SB();
  QK(2, 0, 2); CV(0, 2); VA(5); SB(); QK(2, 1, 2); CV(0, 3); VB(5); SB(); QK(2, 0, 3); EX(1, 0, 0); VA(6); SB(); QK(2, 1, 3); EX(1, 0, 1); VB(6); SB();
  PV(0, 0); EX(1, 0, 2); VA(7); SB(); PV(0, 1); EX(1, 0, 3); VB(7); SB();
  PV(0, 2); EX(1, 1, 0); SB(); PV(0, 3); EX(1, 1, 1); SB(); PV(0, 4); EX(1, 1, 2); SB(); PV(0, 5); EX(1, 1, 3); SB();
  PV(0, 6); CV(1, 0); CV(1, 1); SB(); PV(0, 7); CV(1, 2); CV(1, 3); SB(); LS(0); EX(2, 0, 0); SB();
  PV(1, 0); EX(2, 0, 1); SB(); PV(1, 1); EX(2, 0, 2); SB(); PV(1, 2); EX(2, 0, 3); SB(); PV(1, 3); EX(2, 1, 0); SB();
  PV(1, 4); EX(2, 1, 1); SB(); PV(1, 5); EX(2, 1, 2); SB(); PV(1, 6); EX(2, 1, 3); SB(); PV(1, 7); CV(2, 0); CV(2, 1); SB(); LS(1); CV(2, 2); CV(2, 3); SB();
  bar(); SB();
  PV(2, 0); KR(0, 0); SB(); PV(2, 1); KR(1, 0); SB(); PV(2, 2); KR(0, 1); SB(); PV(2, 3); KR(1, 1); SB();
  PV(2, 4); KR(0, 2); SB(); PV(2, 5); KR(1, 2); SB(); PV(2, 6); KR(0, 3); SB(); PV(2, 7); KR(1, 3); SB();
  LS(2); SB();
#undef QK
#undef EX
#undef CV
#undef VA
#undef VB
#undef KR
#undef PV
#undef LS
}

template <bool SAFE>
DI void p2_compute(LAS char* L, int w, const bf16x8 (&Qf)[4][3], f32x4 (&O)[8][3], float (&m)[3], float (&l)[3]) {
  const unsigned lane = fresh_lane();
  const unsigned gq = lane >> 4, i16 = lane & 15, qp = i16 >> 2, p = lane & 3;
  LAS char* kb = L + 8192u * w + 2048u * (i16 >> 3) + 64u * (i16 & 7) + 16u * (gq ^ sigma((i16 >> 2) & 3));
  LAS char* vb0 = L + SLOT + 8192u * w + 2048u * (gq >> 1) + 256u * (gq & 1) + 64u * qp + 16u * (2u * (0u ^ (gq & 1)) + (p >> 1)) + 8u * (p & 1);
  LAS char* vb1 = L + SLOT + 8192u * w + 2048u * (gq >> 1) + 256u * (gq & 1) + 64u * qp + 16u * (2u * (1u ^ (gq & 1)) + (p >> 1)) + 8u * (p & 1);
  f32x4 negm = {0.f, 0.f, 0.f, 0.f}, Ls[3];
  bf16x8 Kf[2][4];
  bf16x8 ones = asbf(u32x4{0x3f803f80u, 0x3f803f80u, 0x3f803f80u, 0x3f803f80u});
  asm volatile("" : "+v"(ones));
#pragma unroll
  for (int d = 0; d < 8; ++d)
#pragma unroll
    for (int q = 0; q < 3; ++q) O[d][q] = f32x4{0.f, 0.f, 0.f, 0.f};
#pragma unroll
  for (int q = 0; q < 3; ++q) { Ls[q] = f32x4{0.f, 0.f, 0.f, 0.f}; m[q] = -__builtin_inff(); }
#pragma unroll
  for (int X = 0; X < 2; ++X)
#pragma unroll
    for (int s = 0; s < 4; ++s) Kf[X][s] = lds128(kb + 4096 * X + 512 * s);
  int off = 2 * (int)SLOT;
  int i = 0;
  if constexpr (!SAFE) { p2_step_plain<0>(kb + off, vb0, vb1, Qf, Kf, O, Ls, negm, m, ones); kb += off; vb0 += off; vb1 += off; off = -off; i = 1; }
#pragma unroll 1
  for (; i < 16; ++i) {
    if constexpr (!SAFE) p2_step_fast(kb + off, vb0, vb1, Qf, Kf, O, Ls, negm, ones);
    else p2_step_plain<2>(kb + off, vb0, vb1, Qf, Kf, O, Ls, negm, m, ones);
    kb += off; vb0 += off; vb1 += off; off = -off;
  }
  asm volatile("s_nop 7\n\ts_nop 3" ::: "memory");
#pragma unroll
  for (int q = 0; q < 3; ++q) l[q] = Ls[q][0];
}

DI bool ml_bad(LAS char* L, int lane) {
  const LAS float* ml = (const LAS float*)(L + SCR_ML);
  bool bad = false;
#pragma unroll
  for (int t = 0; t < 3; ++t) { const int e = t * 64 + lane; const float v = ml[(e >> 2) * 8 + 4 + (e & 3)]; bad |= !(v < L_LIMIT && v > L_FLOOR); }
  return __any(bad);
}

template <int MODE> DI unsigned xarea(int W, int t) {
  if (MODE == 1) return (unsigned)(t - 1) * SLOT + (unsigned)W * 8192u;
  return t == 1 ? 3u * SLOT + (unsigned)W * 8192u : t == 2 ? 2u * SLOT + (unsigned)W * 8192u : (W < 2 ? SCR_Q + (unsigned)W * 6144u : SCR + 16384u + (unsigned)(W - 2) * 6144u);
}
template <int W, int MODE> DI void scatter_quarters(LAS char* L, int lane, const f32x4 (&T)[8][3], f32x4 (&own)[2][3]) {
#pragma unroll
  for (int t = 1; t < 4; ++t) { const int dst = (W + t) & 3;
#pragma unroll
    for (int X = 0; X < 2; ++X)
#pragma unroll
      for (int q = 0; q < 3; ++q) *(LAS f32x4*)(L + xarea<MODE>(W, t) + (X * 3 + q) * 1024 + lane * 16) = T[2 * dst + X][q]; }
#pragma unroll
  for (int X = 0; X < 2; ++X)
#pragma unroll
    for (int q = 0; q < 3; ++q) own[X][q] = T[2 * W + X][q];
  asm volatile("; quarter %0" :: "i"(W) : "memory");
}
template <int MODE> DI void scatter_q(int w, LAS char* L, int lane, const f32x4 (&T)[8][3], f32x4 (&own)[2][3]) {
  if (w == 0) scatter_quarters<0, MODE>(L, lane, T, own); else if (w == 1) scatter_quarters<1, MODE>(L, lane, T, own);
  else if (w == 2) scatter_quarters<2, MODE>(L, lane, T, own); else scatter_quarters<3, MODE>(L, lane, T, own);
}

__global__ void __launch_bounds__(512, 2)
gqa_block_kernel(const char* __restrict__ x, const char* __restrict__ kc, const char* __restrict__ vc, const char* __restrict__ wq,
                 const char* __restrict__ wo, char* __restrict__ out, char* __restrict__ ws) {
  extern __shared__ __attribute__((aligned(16))) char lds[];
  LAS char* L = (LAS char*)lds;
  const unsigned ldsb = (unsigned)(size_t)L;
  const int wid = (int)rfl(threadIdx.x >> 6), lane = threadIdx.x & 63;
  const int xcd = blockIdx.x & 7, idx = blockIdx.x >> 3;
  const int g = xcd >> 1, bl = idx >> 3, hl = idx & 7, b = 4 * (xcd & 1) + bl, h = 8 * g + hl;
  const __amdgpu_buffer_rsrc_t srdWq = mk_srd(wq, DM * HQ * DH * 2), srdX = mk_srd(x, NB * SQ * DM * 2), srdWo = mk_srd(wo, HQ * DH * DM * 2);
  const __amdgpu_buffer_rsrc_t srdK = mk_srd(kc + (size_t)(b * HKV + g) * SK * DH * 2, SK * DH * 2), srdV = mk_srd(vc + (size_t)(b * HKV + g) * SK * DH * 2, SK * DH * 2);

  if (wid >= 4) {
    const int j = wid - 4;
    const unsigned sub = lane >> 5, r7 = (lane & 31) >> 2, pos = lane & 3;
    const unsigned vW = r7 * 8192u + 64u * sub + 16u * pos;
    const unsigned vX0 = r7 * 2048u + 64u * sub + 16u * (pos ^ sigma((r7 >> 2) & 3)), vX1 = r7 * 2048u + 64u * sub + 16u * (pos ^ sigma((2u + (r7 >> 2)) & 3));
    const unsigned vWo = r7 * 2048u + 64u * sub + 16u * (pos ^ (2u * (r7 >> 2)));
    const unsigned vK0 = r7 * 256u + 64u * sub + 16u * (pos ^ sigma((r7 >> 2) & 3)), vK1 = r7 * 256u + 64u * sub + 16u * (pos ^ sigma((2u + (r7 >> 2)) & 3));
    const unsigned vV = r7 * 256u + 64u * sub + 16u * (pos ^ (2u * (r7 >> 2)));
    const int ev0 = CL0 + ((hl * SG0) >> 3), ev1 = CL1 + ((hl * SG1) >> 3), ev2 = CL2 + ((hl * SG2) >> 3), ev3 = CL3 + ((hl * SG3) >> 3);

    u64 ew;
    { const char* ep = ws + WS_E + 256 * b; asm volatile("s_nop 4\n\tglobal_load_dwordx2 %0, %1, off sc1" : "=v"(ew) : "v"(ep) : "memory"); }
    auto issueP1 = [&](int t) {
      const unsigned base = ldsb + (unsigned)(t & 3) * SLOT;
#pragma unroll
      for (int pp = 0; pp < 4; ++pp) { const unsigned pc = 4u * j + pp; dma16<false>(base + 1024u * pc, vW, srdWq, (64u * t + 8u * (pc >> 1)) * 8192u + h * 256u + 128u * (pc & 1)); }
      if (j < 2) { dma16<false>(base + XOFF + 2048u * j, vX0, srdX, (b * 48u + 16u * j) * 2048u + 128u * t);
                   dma16<false>(base + XOFF + 2048u * j + 1024u, vX1, srdX, (b * 48u + 16u * j + 8u) * 2048u + 128u * t); }
      else dma16<false>(base + XOFF + 1024u * (2 + j), (j & 1) ? vX1 : vX0, srdX, (b * 48u + 8u * (2 + j)) * 2048u + 128u * t);
    };
    auto cluster = [&](int c) { touch(ldsb + SCR_DUMMY, lane * 128u, j < 2 ? srdK : srdV, (512u * c + 64u * hl) * 256u + (j & 1) * 8192u); };
    auto clusters_at = [&](int e) { if (e == ev0) cluster(0); if (e == ev1) cluster(1); if (e == ev2) cluster(2); if (e == ev3) cluster(3); };
    auto issueK = [&](int i) {
      const unsigned base = ldsb + (unsigned)((2 * i) & 3) * SLOT + 8192u * j, k0 = 128u * i + 32u * j;
#pragma unroll
      for (int pc = 0; pc < 8; ++pc) dma16<true>(base + 2048u * (pc >> 1) + 1024u * (pc & 1), ((pc >> 1) & 1) ? vK1 : vK0, srdK, (k0 + 8u * (pc >> 1)) * 256u + 128u * (pc & 1));
    };
    auto issueV = [&](int i) {
      const unsigned base = ldsb + (unsigned)((2 * i + 1) & 3) * SLOT + 8192u * j, k0 = 128u * i + 32u * j;
#pragma unroll
      for (int pc = 0; pc < 8; ++pc) dma16<true>(base + 2048u * (pc >> 1) + 1024u * (pc & 1), vV, srdV, (k0 + 8u * (pc >> 1)) * 256u + 128u * (pc & 1));
    };
    auto issueWo = [&](int u) {
      asm volatile("" : "+s"(u));
      const unsigned base = ldsb + (unsigned)(u & 3) * SLOT + 8192u * j;
      const unsigned ur = (u + 2 * bl) & 7;
      const unsigned so = ((8u * g + 2u * j + (ur >> 2)) * 128u + (ur & 3) * 32u) * 2048u + hl * 256u;
#pragma unroll
      for (int pc = 0; pc < 8; ++pc) dma16<false>(base + 2048u * (pc >> 1) + 1024u * (pc & 1), vWo, srdWo, so + 8u * (pc >> 1) * 2048u + 128u * (pc & 1));
    };
    const __amdgpu_buffer_rsrc_t srdO = mk_srd(ws + WS_O, 256 * 12288), srdP = mk_srd(ws + WS_P, 256 * 24576);
    auto clear1 = [&](const __amdgpu_buffer_rsrc_t& srd, unsigned so) {
      u32x4 pz = {POISON, POISON, POISON, POISON};
      asm volatile("buffer_store_dwordx4 %0, %1, %2, %3 offen sc1\n\ts_nop 1" :: "v"(pz), "v"((unsigned)lane * 16u), "s"(srd), "s"(so) : "memory");
    };
    const int clr_at = min(max(ev2 - 16, 0), 10);
    auto kv_run = [&](const bool warm) {
#pragma unroll 1
      for (int i = 0; i < 14; ++i) {
        const bool flag = warm && i == clr_at + 2;
        if (flag) wait_vm<0>();
        bar();
        if (flag && j == 0) {
          const u64 tg = *(const LAS u64*)(L + SY_TAG);
          if (lane == 0) { const char* cp = ws + WS_C + (b * 32 + h) * 8; asm volatile("global_store_dwordx2 %0, %1, off sc1\n\ts_nop 1" :: "v"(cp), "v"(tg) : "memory"); }
        }
        issueK(i + 2);
        if (warm) clusters_at(16 + i);
        wait_vm<8>();
        bar(); issueV(i + 2);
        if (warm && i == clr_at) {
#pragma unroll
          for (int q = 0; q < 3; ++q) clear1(srdO, (unsigned)((b * 32 + h) * 12288 + (j * 3 + q) * 1024));
          if (j != g) {
#pragma unroll
            for (int e = 0; e < 6; ++e) clear1(srdP, (unsigned)((((b * 8 + hl) * 4 + g) * 4 + j) * 6144 + e * 1024));
          }
        }
      }
      bar(); issueWo(0);
      wait_vm<8>(); bar(); issueWo(1);
      bar(); bar();
    };
    issueP1(0); issueP1(1); issueP1(2);
    if (j < 2) wait_vm<12>(); else wait_vm<10>();
    asm volatile("" : "+v"(ew));
    if (j == 0 && lane == 0) { *(LAS u64*)(L + SY_TAG) = ew + 1ull; asm volatile("s_waitcnt lgkmcnt(0)" ::: "memory"); }
    bar(); issueP1(3);
#pragma unroll 1
    for (int t = 1; t < 13; ++t) {
      if (j < 2) wait_vm<12>(); else wait_vm<10>();
      bar(); issueP1(t + 3);
      clusters_at(t);
    }
    if (j < 2) wait_vm<12>(); else wait_vm<10>();
    bar(); issueK(0); clusters_at(13);
    if (j < 2) wait_vm<14>(); else wait_vm<13>();
    bar(); issueV(0); clusters_at(14);
    wait_vm<16>(); bar(); issueK(1); clusters_at(15);
    wait_vm<8>(); bar(); issueV(1);
    kv_run(true);
    bar();
    if (ml_bad(L, lane)) {
      wait_vm<0>();
      bar();
      issueK(0); issueV(0); issueK(1);
      wait_vm<8>(); bar(); issueV(1);
      kv_run(false);
      bar();
    }
    bar();
    issueWo(2); issueWo(3);
    bar();
    wait_vm<24>(); bar();
    wait_vm<16>(); bar(); issueWo(4);
#pragma unroll 1
    for (int u = 5; u < 8; ++u) { wait_vm<16>(); bar(); issueWo(u); }
    wait_vm<16>(); bar();
    wait_vm<8>(); bar();
    wait_vm<0>(); bar();
    return;
  }

  const int w = wid;
  const unsigned gq = lane >> 4, i16 = lane & 15, qp = i16 >> 2, p = lane & 3;
  f32x4 QA[2][3];
#pragma unroll
  for (int X = 0; X < 2; ++X)
#pragma unroll
    for (int q = 0; q < 3; ++q) QA[X][q] = f32x4{0.f, 0.f, 0.f, 0.f};
  {
    LAS char* xa = L + XOFF + 1024u * (i16 >> 3) + 64u * (i16 & 7) + 16u * (gq ^ sigma((i16 >> 2) & 3));
    LAS char* wa = L + 2048u * gq + 512u * w + 64u * qp + 16u * p;
    bf16x8 Bx[2][2][3], Aw[2][2][2];
    auto rd = [&](const int bf, const unsigned so) {
#pragma unroll
      for (int ks = 0; ks < 2; ++ks) {
#pragma unroll
        for (int X = 0; X < 2; ++X) Aw[bf][ks][X] = ldstr2(wa + so + 8192 * ks + 8 * X, wa + so + 8192 * ks + 256 + 8 * X);
#pragma unroll
        for (int q = 0; q < 3; ++q) Bx[bf][ks][q] = lds128(xa + so + 2048 * q + 512 * ks);
      }
    };
    auto mm = [&](const int bf) {
#pragma unroll
      for (int ks = 0; ks < 2; ++ks)
#pragma unroll
        for (int X = 0; X < 2; ++X)
#pragma unroll
          for (int q = 0; q < 3; ++q) QA[X][q] = mfma(Aw[bf][ks][X], Bx[bf][ks][q], QA[X][q]);
    };
    bar_lgkm(); rd(0, 0u); SB();
#pragma unroll 1
    for (int t = 1; t < 15; t += 2) {
      bar_lgkm(); rd(1, (unsigned)(t & 3) * SLOT); SB(); mm(0); SB();
      bar_lgkm(); rd(0, (unsigned)((t + 1) & 3) * SLOT); SB(); mm(1); SB();
    }
    bar_lgkm(); rd(1, 3u * SLOT); SB(); mm(0); SB();
    mm(1);
  }
#pragma unroll
  for (int q = 0; q < 3; ++q) *(LAS bf16x8*)(L + SCR_Q + (w * 3 + q) * 1024 + lane * 16) = pack8(QA[0][q] * QSCALE, QA[1][q] * QSCALE);
  bar_lgkm();
  bf16x8 Qf[4][3];
#pragma unroll
  for (int s = 0; s < 4; ++s)
#pragma unroll
    for (int q = 0; q < 3; ++q) Qf[s][q] = lds128(L + SCR_Q + (s * 3 + q) * 1024 + lane * 16);

  f32x4 O[8][3];
  float m[3], l[3];
  p2_compute<false>(L, w, Qf, O, m, l);
  const int lane2 = fresh_lane();
  const unsigned gq2 = lane2 >> 4, i2 = lane2 & 15;
  LAS float* ml = (LAS float*)(L + SCR_ML);
  f32x4 Own[2][3];
  auto publish_partials = [&]() {
    scatter_q<0>(w, L, lane2, O, Own);
    if (gq2 == 0) {
#pragma unroll
      for (int q = 0; q < 3; ++q) { ml[(q * 16 + i2) * 8 + w] = m[q]; ml[(q * 16 + i2) * 8 + 4 + w] = l[q]; } }
  };
  const u64 tag0 = *(const LAS u64*)(L + SY_TAG);
  const u64 tag = ((u64)rfl((unsigned)(tag0 >> 32)) << 32) | rfl((unsigned)tag0);
  const bool cmine = lane2 < 2 || (w == g && lane2 < 5);
  const u64* cptr = (const u64*)(ws + WS_C) + b * 32 + (lane2 < 2 ? 8 * g + 2 * w + lane2 : 8 * ((g + lane2 - 1) & 3) + hl);
  const u64 cf0 = cmine ? ld_flag(cptr) : tag;
  publish_partials();
  bar_lgkm();
  if (ml_bad(L, lane2)) {
    bar_lgkm();
    bar();
    p2_compute<true>(L, w, Qf, O, m, l);
    publish_partials();
    bar_lgkm();
  }
  f32x4 Oq[2][3];
  {
    float f[4][3];
#pragma unroll
    for (int q = 0; q < 3; ++q) {
      const f32x4 mm = *(const LAS f32x4*)(ml + (q * 16 + i2) * 8), ll = *(const LAS f32x4*)(ml + (q * 16 + i2) * 8 + 4);
      const float M = fmaxf(fmaxf(mm[0], mm[1]), fmaxf(mm[2], mm[3]));
      const f32x4 e = {ex2(mm[0] - M), ex2(mm[1] - M), ex2(mm[2] - M), ex2(mm[3] - M)};
      const float inv = __builtin_amdgcn_rcpf((e[0] * ll[0] + e[1] * ll[1]) + (e[2] * ll[2] + e[3] * ll[3]));
      const f32x4 r1 = (w & 1) ? f32x4{e[1], e[0], e[3], e[2]} : e;
      const f32x4 r2 = (w & 2) ? f32x4{r1[2], r1[3], r1[0], r1[1]} : r1;
      f[0][q] = r2[0] * inv; f[2][q] = r2[2] * inv;
      f[1][q] = ((w & 1) ? r2[1] : r2[3]) * inv; f[3][q] = ((w & 1) ? r2[3] : r2[1]) * inv;
    }
#pragma unroll
    for (int X = 0; X < 2; ++X)
#pragma unroll
      for (int q = 0; q < 3; ++q) {
        f32x4 a = Own[X][q] * f[0][q];
#pragma unroll
        for (int t = 1; t < 4; ++t) {
          const f32x4 v = *(const LAS f32x4*)(L + xarea<0>((w - t) & 3, t) + (X * 3 + q) * 1024 + lane2 * 16);
          a += v * f[t][q]; }
        Oq[X][q] = a;
      }
  }
  const __amdgpu_buffer_rsrc_t srdO = mk_srd(ws + WS_O, 256 * 12288), srdP = mk_srd(ws + WS_P, 256 * 24576);
#pragma unroll
  for (int q = 0; q < 3; ++q)
    __builtin_amdgcn_raw_buffer_store_b128(sanitize(__builtin_bit_cast(u32x4, pack8(Oq[0][q], Oq[1][q]))), srdO, (unsigned)((b * 32 + h) * 12288 + (w * 3 + q) * 1024 + lane2 * 16), 0, 16);
  bar_lgkm();
  wait_words(cf0, cptr, cmine, tag, lane2, ws);
  u32x4 Of[8][3];
  for (int d = 0; d < PDELAY; ++d) __builtin_amdgcn_s_sleep(4);
  {
    const unsigned so = (unsigned)((b * 32 + 8 * g + 2 * w + ((lane2 >> 2) & 1)) * 12288 + ((lane2 & 3) * 3 + 2) * 1024 + 1020);
#pragma unroll 1
    for (int spins = 0; spins < SPIN_CAP; ++spins) {
      asm volatile("" ::: "memory");
      const unsigned v = __builtin_amdgcn_raw_buffer_load_b32(srdO, lane2 < 8 ? so : 0u, 0, 16);
      if (!__any(lane2 < 8 && v == POISON)) break;
    }
  }
  for (int spins = 0;; ++spins) {
    asm volatile("" ::: "memory");
#pragma unroll
    for (int u = 0; u < 8; ++u) {
      const int ur = (u + 2 * bl) & 7;
#pragma unroll
      for (int q = 0; q < 3; ++q)
        Of[u][q] = __builtin_amdgcn_raw_buffer_load_b128(srdO, (unsigned)(lane2 * 16 + q * 1024), (unsigned)((b * 32 + 8 * g + 2 * w + (ur >> 2)) * 12288 + (ur & 3) * 3072), 16);
    }
    if (spins == 0) bar();
    unsigned mx = 0;
#pragma unroll
    for (int u = 0; u < 8; ++u) mx = max4(max4(max4(mx, Of[u][0]), Of[u][1]), Of[u][2]);
    if (__builtin_expect(!__any(mx == POISON), 1)) break;
    if (spins > SPIN_CAP) { if (lane2 == 0) st_flag((u64*)(ws + WS_TMO), 1ull); break; }
  }
  f32x4 A[8][3];
#pragma unroll
  for (int d = 0; d < 8; ++d)
#pragma unroll
    for (int q = 0; q < 3; ++q) { A[d][q] = f32x4{0.f, 0.f, 0.f, 0.f}; asm volatile("" : "+v"(A[d][q])); }
  {
    const unsigned qp2 = i2 >> 2, p2 = lane2 & 3;
    LAS char* vb0 = L + 8192u * w + 2048u * (gq2 >> 1) + 256u * (gq2 & 1) + 64u * qp2 + 16u * (2u * (0u ^ (gq2 & 1)) + (p2 >> 1)) + 8u * (p2 & 1);
    LAS char* vb1 = L + 8192u * w + 2048u * (gq2 >> 1) + 256u * (gq2 & 1) + 64u * qp2 + 16u * (2u * (1u ^ (gq2 & 1)) + (p2 >> 1)) + 8u * (p2 & 1);
    bf16x4 Wa[8], Wb[8];
    auto rd1 = [&](const int nu, const int d, const int hf) {
      LAS char* a = ((d & 1) ? vb1 : vb0) + (nu & 3) * SLOT + 512 * (d >> 1) + 4096 * hf;
      if (hf == 0) Wa[d] = __builtin_amdgcn_ds_read_tr16_b64_v4bf16((LAS bf16x4*)a); else Wb[d] = __builtin_amdgcn_ds_read_tr16_b64_v4bf16((LAS bf16x4*)a);
    };
    auto half = [&](const int u, const int d0, const bool more, const int nu, const int nd0) {
#pragma unroll
      for (int e = 0; e < 12; ++e) {
        const int d = d0 + e / 3, q = e % 3;
        mfma_acc(A[d][q], __builtin_shufflevector(Wa[d], Wb[d], 0, 1, 2, 3, 4, 5, 6, 7), asbf(Of[u][q]));
        if (more && e < 8) rd1(nu, nd0 + (e >> 1), e & 1);
        SB();
      }
    };
    bar();
#pragma unroll
    for (int e = 0; e < 8; ++e) rd1(0, e >> 1, e & 1);
    SB();
#pragma unroll
    for (int u = 0; u < 8; ++u) {
      half(u, 0, true, u, 4);
      if (u < 7) bar_lgkm();
      half(u, 4, u < 7, u + 1, 0);
    }
    asm volatile("s_nop 7\n\ts_nop 3" ::: "memory");
  }
  f32x4 R[2][3];
  scatter_q<1>(w, L, lane2, A, R);
  bar_lgkm();
#pragma unroll
  for (int X = 0; X < 2; ++X)
#pragma unroll
    for (int q = 0; q < 3; ++q) {
      f32x4 a = R[X][q];
#pragma unroll
      for (int t = 1; t < 4; ++t) a += *(const LAS f32x4*)(L + (t - 1) * SLOT + ((w - t) & 3) * 8192 + (X * 3 + q) * 1024 + lane2 * 16);
      R[X][q] = a;
    }
  if (w != g) {
#pragma unroll
    for (int X = 0; X < 2; ++X)
#pragma unroll
      for (int q = 0; q < 3; ++q)
        __builtin_amdgcn_raw_buffer_store_b128(sanitize(__builtin_bit_cast(u32x4, R[X][q])), srdP, (unsigned)((((b * 8 + hl) * 4 + g) * 4 + w) * 6144 + (X * 3 + q) * 1024 + lane2 * 16), 0, 16);
    return;
  }
  u32x4 Rin[3][6];
  for (int d = 0; d < PDELAY2; ++d) __builtin_amdgcn_s_sleep(4);
  {
    const unsigned so = (unsigned)((((b * 8 + hl) * 4 + ((g + 1 + (lane2 & 3)) & 3)) * 4 + g) * 6144 + 5 * 1024 + 1020);
#pragma unroll 1
    for (int spins = 0; spins < SPIN_CAP; ++spins) {
      asm volatile("" ::: "memory");
      const unsigned v = __builtin_amdgcn_raw_buffer_load_b32(srdP, lane2 < 3 ? so : 0u, 0, 16);
      if (!__any(lane2 < 3 && v == POISON)) break;
    }
  }
  for (int spins = 0;; ++spins) {
    asm volatile("" ::: "memory");
    unsigned mx = 0;
#pragma unroll
    for (int t = 0; t < 3; ++t)
#pragma unroll
      for (int e = 0; e < 6; ++e) {
        Rin[t][e] = __builtin_amdgcn_raw_buffer_load_b128(srdP, (unsigned)(lane2 * 16 + e * 1024), (unsigned)((((b * 8 + hl) * 4 + ((g + t + 1) & 3)) * 4 + g) * 6144), 16);
        mx = max4(mx, Rin[t][e]);
      }
    if (__builtin_expect(!__any(mx == POISON), 1)) break;
    if (spins > SPIN_CAP) { if (lane2 == 0) st_flag((u64*)(ws + WS_TMO), 1ull); break; }
  }
  if (g == 0 && hl == 0 && lane2 == 0) st_flag((u64*)(ws + WS_E + 256 * b), tag);
#pragma unroll
  for (int X = 0; X < 2; ++X)
#pragma unroll
    for (int q = 0; q < 3; ++q) R[X][q] += (__builtin_bit_cast(f32x4, Rin[0][X * 3 + q]) + __builtin_bit_cast(f32x4, Rin[1][X * 3 + q])) + __builtin_bit_cast(f32x4, Rin[2][X * 3 + q]);
#pragma unroll
  for (int X = 0; X < 2; ++X)
#pragma unroll
    for (int q = 0; q < 3; ++q) {
      u32x2 o2 = {cvtpk(R[X][q][0], R[X][q][1]), cvtpk(R[X][q][2], R[X][q][3])};
      *(u32x2*)(out + (size_t)(b * 48 + 16 * q + i2) * 2048 + (128 * hl + 32 * g + 16 * X + 4 * gq2) * 2) = o2;
    }
}

extern "C" void kernel_launch(void* const* d_in, const int* in_sizes, int n_in, void* d_out, int out_size, void* d_ws, size_t ws_size, hipStream_t stream) {
  (void)in_sizes; (void)n_in; (void)out_size; (void)ws_size;
  (void)hipFuncSetAttribute((const void*)gqa_block_kernel, hipFuncAttributeMaxDynamicSharedMemorySize, (int)LDS_BYTES);
  hipLaunchKernelGGL(gqa_block_kernel, dim3(256), dim3(512), LDS_BYTES, stream,
                     (const char*)d_in[0], (const char*)d_in[1], (const char*)d_in[2], (const char*)d_in[3], (const char*)d_in[4], (char*)d_out, (char*)d_ws);
}
```
